```python
import torch

def _softmax(x, axis=-1):
    return torch.softmax(x, dim=axis)
def _gelu(x):
    return torch.nn.functional.gelu(x, approximate="tanh")
def _silu(x):
    return torch.nn.functional.silu(x)
def _sigmoid(x):
    return torch.sigmoid(x)
def _layernorm(x, axis=-1, eps=1e-5):
    m = x.mean(dim=axis, keepdim=True)
    v = x.var(dim=axis, keepdim=True, unbiased=False)
    return (x - m) / torch.sqrt(v + eps)
def _scatter_add(tgt, idx, src, axis=0):
    return tgt.clone().index_add_(axis, idx, src)
def _segment_sum(src, seg, n):
    out = torch.zeros((n,) + src.shape[1:], dtype=src.dtype)
    return out.index_add_(0, seg, src)
def _pad(x, pw):
    flat = tuple(v for pair in reversed(pw) for v in pair)
    return torch.nn.functional.pad(x, flat)

kernel_name = 'parallel_sum__grouped_kv_mqa__swiglu_ffn__prompt'

def setup_inputs():

    _w = (lambda a: (lambda t: t.to(torch.bfloat16) if t.is_floating_point() else t)(torch.as_tensor(a)))
    def _A_setup():
        B, S, D, H = 1, 2048, 640, 32
        Dh = D // H
        return {
            "x": _w((lambda *s: torch.randn(*s, dtype=torch.bfloat16) * 0.05)(B, S, D)),
            "wq": _w((lambda *s: torch.randn(*s, dtype=torch.bfloat16) * 0.2)(D, H * Dh)),
            "wk": _w((lambda *s: torch.randn(*s, dtype=torch.bfloat16) * 0.2)(D, Dh)),
            "wv": _w((lambda *s: torch.randn(*s, dtype=torch.bfloat16) * 0.1)(D, Dh)),
            "wo": _w((lambda *s: torch.randn(*s, dtype=torch.bfloat16) * 0.1)(H * Dh, D)),
        }
    _A_in = _A_setup()

    def _B_setup():
        B, S, D, F = 1, 2048, 640, 1728
        return {
            "x": _w((lambda *s: torch.randn(*s, dtype=torch.bfloat16) * 0.05)(B, S, D)),
            "w_gate": _w((lambda *s: torch.randn(*s, dtype=torch.bfloat16) * 0.1)(D, F)),
            "w_up": _w((lambda *s: torch.randn(*s, dtype=torch.bfloat16) * 0.1)(D, F)),
            "w_down": _w((lambda *s: torch.randn(*s, dtype=torch.bfloat16) * 0.1)(F, D)),
        }
    _B_in = _B_setup()

    return {
        "A_x": _A_in["x"],
        "A_wq": _A_in["wq"],
        "A_wk": _A_in["wk"],
        "A_wv": _A_in["wv"],
        "A_wo": _A_in["wo"],
        "B_x": _B_in["x"],
        "B_w_gate": _B_in["w_gate"],
        "B_w_up": _B_in["w_up"],
        "B_w_down": _B_in["w_down"],
    }

def reference(A_x, A_wq, A_wk, A_wv, A_wo, B_x, B_w_gate, B_w_up, B_w_down):
    def _A_ref(x, wq, wk, wv, wo):
        B, S, D = x.shape
        H = 32
        Dh = D // H
        q = (lambda x, *d: x.permute(*d))((x @ wq).reshape(B, S, H, Dh), 0, 2, 1, 3)
        k = x @ wk
        v = x @ wv
        scores = torch.einsum("bhqd,bkd->bhqk", q, k) * (Dh ** -0.5)
        p = _softmax(scores, axis=-1)
        o = torch.einsum("bhqk,bkd->bhqd", p, v)
        return (lambda x, *d: x.permute(*d))(o, 0, 2, 1, 3).reshape(B, S, H * Dh) @ wo

    def _B_ref(x, w_gate, w_up, w_down):
        g = _silu(x @ w_gate)
        u = x @ w_up
        return (g * u) @ w_down

    x = A_x
    xn = _layernorm(x, axis=-1)
    return x + _A_ref(xn, A_wq, A_wk, A_wv, A_wo) + _B_ref(xn, B_w_gate, B_w_up, B_w_down)
```

```cpp
#include <hip/hip_runtime.h>
#include <hip/hip_bf16.h>

typedef unsigned short u16;
typedef __attribute__((ext_vector_type(8))) __bf16 bf16x8;
typedef __attribute__((ext_vector_type(2))) __bf16 bf16x2;
typedef __attribute__((ext_vector_type(4))) short s16x4;
typedef __attribute__((ext_vector_type(8))) short s16x8;
typedef __attribute__((ext_vector_type(16))) float f32x16;
typedef __attribute__((ext_vector_type(4))) float f32x4;
typedef __attribute__((ext_vector_type(2))) float f32x2;
typedef __attribute__((ext_vector_type(4))) unsigned u32x4;
typedef __attribute__((ext_vector_type(2))) unsigned u32x2;
typedef __attribute__((address_space(3))) s16x4* lds_s16x4_p;
typedef __attribute__((address_space(3))) void* lds_void_p;

constexpr int DM = 640, FF = 1728;
constexpr int NKS3 = 148;
constexpr size_t XN_OFF  = 0;
constexpr size_t QF0_OFF = XN_OFF + 64 * 40 * 1024;
constexpr size_t QF1_OFF = QF0_OFF + 64 * 32 * 1024;
constexpr size_t KV_OFF  = QF1_OFF + 64 * 32 * 256;
constexpr size_t ACT_OFF = KV_OFF + 64 * 4096;

#define DEVI __device__ __forceinline__
DEVI unsigned pk2(float a, float b) { f32x2 v = {a, b}; return __builtin_bit_cast(unsigned, __builtin_convertvector(v, bf16x2)); }
DEVI float bflo(unsigned u) { return __builtin_bit_cast(float, u << 16); }
DEVI float bfhi(unsigned u) { return __builtin_bit_cast(float, u & 0xffff0000u); }
DEVI void glds16(const void* g, char* l) { __builtin_amdgcn_global_load_lds(g, (lds_void_p)l, 16, 0, 0); }
DEVI s16x4 trrd(const char* l) { return __builtin_amdgcn_ds_read_tr16_b64_v4i16((lds_s16x4_p)l); }
DEVI bf16x8 cat(s16x4 a, s16x4 b) { return __builtin_bit_cast(bf16x8, __builtin_shufflevector(a, b, 0, 1, 2, 3, 4, 5, 6, 7)); }
DEVI f32x16 mfma32(bf16x8 a, bf16x8 b, f32x16 c) { return __builtin_amdgcn_mfma_f32_32x32x16_bf16(a, b, c, 0, 0, 0); }
DEVI f32x4 mfma16(bf16x8 a, bf16x8 b, f32x4 c) { return __builtin_amdgcn_mfma_f32_16x16x32_bf16(a, b, c, 0, 0, 0); }
DEVI f32x16 mfma32x8(u32x2 a, u32x2 b, f32x16 c) { return __builtin_amdgcn_mfma_f32_32x32x8bf16_1k(__builtin_bit_cast(s16x4, a), __builtin_bit_cast(s16x4, b), c, 0, 0, 0); }
DEVI float xhalf(float v) { return __shfl_xor(v, 32); }
#define WAITV(n) asm volatile("s_waitcnt vmcnt(" #n ")" ::: "memory")
#define BAR() do { asm volatile("s_waitcnt lgkmcnt(0)" ::: "memory"); __builtin_amdgcn_s_barrier(); asm volatile("" ::: "memory"); } while (0)
DEVI u32x4 pack8(const f32x16& a, int o, float s) {
  u32x4 r; r[0] = pk2(a[o] * s, a[o + 1] * s); r[1] = pk2(a[o + 2] * s, a[o + 3] * s); r[2] = pk2(a[o + 4] * s, a[o + 5] * s); r[3] = pk2(a[o + 6] * s, a[o + 7] * s); return r;
}
DEVI u32x2 pack4(const f32x16& a, int o, float s) { u32x2 r; r[0] = pk2(a[o] * s, a[o + 1] * s); r[1] = pk2(a[o + 2] * s, a[o + 3] * s); return r; }

constexpr int K1_EXTRA = 102400, K1_STAT = 122880, K1_LDS = 123904;
__global__ __launch_bounds__(256, 2) void k1_ln_qkv(const u16* __restrict__ x, const u16* __restrict__ wq, const u16* __restrict__ wk,
                                                    const u16* __restrict__ wv, char* __restrict__ ws) {
  extern __shared__ __attribute__((aligned(16))) char sm[];
  const int tid = threadIdx.x, lane = tid & 63, w = __builtin_amdgcn_readfirstlane(tid >> 6);
  const int b = blockIdx.x, ii = b >> 3, rb = (b & 7) * 4 + (ii & 3), c = ii >> 2;
  const int mtl = w & 1, kh = w >> 1, m = lane & 31, hf = lane >> 5, mt = rb * 2 + mtl;
  u32x4 xf[20];
  {
    const u16* xp = x + (size_t)(mt * 32 + m) * DM + kh * 320 + hf * 8;
#pragma unroll
    for (int s = 0; s < 20; s++) xf[s] = *(const u32x4*)(xp + 16 * s);
  }
  u32x2 ex[10];
  if (c < 3) {
#pragma unroll
    for (int j = 0; j < 10; j++) {
      int id = tid + 256 * j, r = id >> 2, p = id & 3;
      const u16* src = c == 0 ? wk + r * 20 + 4 * p : c == 1 ? (p == 0 ? wk + r * 20 + 16 : wv + r * 20 + 4 * (p - 1)) : wv + r * 20 + 12 + 4 * (p & 1);
      ex[j] = *(const u32x2*)src;
    }
  }
#pragma unroll 5
  for (int j = 0; j < 25; j++) {
    int i = w + 4 * j, id = i * 64 + lane, r = id / 10, ch = id - r * 10;
    glds16(wq + (size_t)r * DM + c * 80 + ch * 8, sm + i * 1024);
  }
  float s1 = 0.f, s2 = 0.f, s1b = 0.f, s2b = 0.f;
  const bf16x2 one2 = {(__bf16)1.0f, (__bf16)1.0f};
#pragma unroll
  for (int s = 0; s < 20; s++)
#pragma unroll
    for (int j = 0; j < 4; j += 2) {
      const unsigned u0 = xf[s][j], u1 = xf[s][j + 1];
      const bf16x2 v0 = __builtin_bit_cast(bf16x2, u0), v1 = __builtin_bit_cast(bf16x2, u1);
      s1 = __builtin_amdgcn_fdot2_f32_bf16(v0, one2, s1, false);
      s2 = __builtin_amdgcn_fdot2_f32_bf16(v0, v0, s2, false);
      s1b = __builtin_amdgcn_fdot2_f32_bf16(v1, one2, s1b, false);
      s2b = __builtin_amdgcn_fdot2_f32_bf16(v1, v1, s2b, false);
    }
  s1 += s1b; s2 += s2b;
  s1 += xhalf(s1); s2 += xhalf(s2);
  if (hf == 0) *(f32x2*)(sm + K1_STAT + (w * 32 + m) * 8) = f32x2{s1, s2};
  if (c < 3) {
#pragma unroll
    for (int j = 0; j < 10; j++) *(u32x2*)(sm + K1_EXTRA + (tid + 256 * j) * 8) = ex[j];
  }
  WAITV(0);
  BAR();
  {
    f32x2 o = *(const f32x2*)(sm + K1_STAT + ((w ^ 2) * 32 + m) * 8);
    s1 += o[0]; s2 += o[1];
  }
  const float mean = s1 * (1.0f / DM);
  const float var = fmaxf(s2 * (1.0f / DM) - mean * mean, 0.f);
  const float rstd = 1.0f / sqrtf(var + 1e-5f), nb = -mean * rstd;
  const int q = (lane & 15) >> 2, p = lane & 3, blk = (lane >> 4) & 1;
  const int r0 = kh * 320 + 8 * hf + q;
  const char* a0 = sm + r0 * 160 + blk * 40 + 8 * p;
  const char* a2 = blk ? sm + K1_EXTRA + r0 * 32 + 8 * p : sm + r0 * 160 + 40 * p + 32;
  const int st2 = blk ? 128 : 640;
  f32x16 acc[3];
#pragma unroll
  for (int t = 0; t < 3; t++)
#pragma unroll
    for (int j = 0; j < 16; j++) acc[t][j] = 0.f;
  char* xn = ws + XN_OFF + (size_t)(mt * 40 + kh * 20) * 1024 + lane * 16;
#pragma unroll
  for (int s = 0; s < 20; s++) {
    u32x4 v;
#pragma unroll
    for (int j = 0; j < 4; j++) v[j] = pk2(fmaf(bflo(xf[s][j]), rstd, nb), fmaf(bfhi(xf[s][j]), rstd, nb));
    if (((kh * 20 + s) & 7) == c) *(u32x4*)(xn + s * 1024) = v;
    bf16x8 bv = __builtin_bit_cast(bf16x8, v);
    acc[0] = mfma32(cat(trrd(a0 + s * 2560), trrd(a0 + s * 2560 + 640)), bv, acc[0]);
    acc[1] = mfma32(cat(trrd(a0 + s * 2560 + 80), trrd(a0 + s * 2560 + 720)), bv, acc[1]);
    acc[2] = mfma32(cat(trrd(a2 + s * 4 * st2), trrd(a2 + (s * 4 + 1) * st2)), bv, acc[2]);
  }
  BAR();
  char* red = sm + mtl * 12288 + lane * 16;
#pragma unroll
  for (int t = 0; t < 3; t++)
    if ((t == 1) != (kh == 1)) {
#pragma unroll
      for (int g = 0; g < 4; g++) *(f32x4*)(red + t * 4096 + g * 1024) = f32x4{acc[t][4 * g], acc[t][4 * g + 1], acc[t][4 * g + 2], acc[t][4 * g + 3]};
    }
  BAR();
#pragma unroll
  for (int t = 0; t < 3; t++)
    if ((t == 1) == (kh == 1)) {
#pragma unroll
      for (int g = 0; g < 4; g++) {
        f32x4 o = *(const f32x4*)(red + t * 4096 + g * 1024);
#pragma unroll
        for (int j = 0; j < 4; j++) acc[t][4 * g + j] += o[j];
      }
    }
  const float QS = 0.22360679774997896f * 1.4426950408889634f;
  char* qf0 = ws + QF0_OFF + (size_t)(mt * 32 + 4 * c) * 1024 + lane * 16;
  char* kv = ws + KV_OFF + (size_t)mt * 4096;
  if (kh == 1) {
    *(u32x4*)(qf0 + 2048) = pack8(acc[1], 0, QS);
    *(u32x4*)(qf0 + 3072) = pack8(acc[1], 8, QS);
    if (c == 3) {
      if (lane < 48) {
        int d = 20 + lane % 12, z = lane / 12;
        unsigned v = (d == 24 || d == 28) ? 0x3f803f80u : 0u;
        *(u32x4*)(kv + 2048 + (z >> 1) * 1024 + (d + 32 * (z & 1)) * 16) = u32x4{v, v, v, v};
      }
      if (lane < 32) *(u32x2*)(kv + 1024 + 256 + lane * 8) = u32x2{0x3f803f80u, 0x00003f80u};
    }
  } else {
    *(u32x4*)(qf0) = pack8(acc[0], 0, QS);
    *(u32x4*)(qf0 + 1024) = pack8(acc[0], 8, QS);
    char* qf1 = ws + QF1_OFF + (size_t)(mt * 32 + 4 * c + hf) * 256 + m * 8;
    *(u32x2*)(qf1) = pack4(acc[2], 0, QS);
    *(u32x2*)(qf1 + 512) = pack4(acc[2], 4, QS);
    if (c == 0) {
      *(u32x4*)(kv + lane * 16) = pack8(acc[2], 8, 1.0f);
    } else if (c < 3) {
      u32x2 lo = pack4(acc[2], 8, 1.0f), hi = pack4(acc[2], 12, 1.0f);
      const int kk = m & 15;
      char* vb = kv + 2048 + (m >> 4) * 1024 + ((kk >> 2) & 1) * 512 + ((kk & 3) + 4 * (kk >> 3)) * 2;
      if (c == 1 && hf == 0) *(u32x2*)(kv + 1024 + m * 8) = lo;
      if (c == 2 || hf == 1) {
        int d = c == 1 ? 0 : 12 + 4 * hf;
        *(u16*)(vb + d * 16) = (u16)lo[0]; *(u16*)(vb + d * 16 + 16) = (u16)(lo[0] >> 16);
        *(u16*)(vb + d * 16 + 32) = (u16)lo[1]; *(u16*)(vb + d * 16 + 48) = (u16)(lo[1] >> 16);
      }
      if (c == 1) {
        int d = 4 + 4 * hf;
        *(u16*)(vb + d * 16) = (u16)hi[0]; *(u16*)(vb + d * 16 + 16) = (u16)(hi[0] >> 16);
        *(u16*)(vb + d * 16 + 32) = (u16)hi[1]; *(u16*)(vb + d * 16 + 48) = (u16)(hi[1] >> 16);
      }
    }
  }
}

constexpr float ABIAS = 64.f;
DEVI u32x2 ext_of(float m) {
  float n = -m;
  unsigned h = __builtin_bit_cast(unsigned, n) & 0xffff0000u; float r = n - __builtin_bit_cast(float, h);
  unsigned md = __builtin_bit_cast(unsigned, r) & 0xffff0000u; float r2 = r - __builtin_bit_cast(float, md);
  return u32x2{(h >> 16) | md, __builtin_bit_cast(unsigned, r2) >> 16};
}
DEVI float max16(const f32x16& s) {
  float a = fmaxf(fmaxf(s[0], s[1]), s[2]);
#pragma unroll
  for (int j = 3; j < 15; j += 2) a = fmaxf(fmaxf(a, s[j]), s[j + 1]);
  return fmaxf(a, s[15]);
}
DEVI void pv_step(f32x16& s, f32x16& o, bf16x8 v0, bf16x8 v1) {
#pragma unroll
  for (int j = 0; j < 16; j++) s[j] = __builtin_amdgcn_exp2f(s[j]);
  o = mfma32(v0, __builtin_bit_cast(bf16x8, pack8(s, 0, 1.0f)), o);
  o = mfma32(v1, __builtin_bit_cast(bf16x8, pack8(s, 8, 1.0f)), o);
}
DEVI void attn_block(int b, char* __restrict__ ws, char* sm) {
  const int tid = threadIdx.x, lane = tid & 63, w = __builtin_amdgcn_readfirstlane(tid >> 6);
  const int ii = b >> 3, mt = (b & 7) * 8 + (ii & 7), head = (ii >> 3) * 8 + w;
  const int m = lane & 31, hf = lane >> 5;
  const char* kvg = ws + KV_OFF;
  const char* src = kvg + w * 4096 + lane * 16;
  char* dst = sm + w * 4096;
#pragma unroll
  for (int i = 0; i < 4; i++) glds16(src + i * 1024, dst + i * 1024);
  const bf16x8 q0 = *(const bf16x8*)(ws + QF0_OFF + (size_t)(mt * 32 + head) * 1024 + lane * 16);
  const u32x2 q1r = *(const u32x2*)(ws + QF1_OFF + (size_t)(mt * 32 + head) * 256 + m * 8);
  u32x2 q1 = hf ? u32x2{0u, 0u} : q1r;
  f32x16 z, o;
#pragma unroll
  for (int j = 0; j < 16; j++) { z[j] = 0.f; o[j] = 0.f; }
  float mm = 0.f;
  const char* l16 = sm + lane * 16;
  const char* l8 = sm + 1024 + lane * 8;
#pragma unroll 1
  for (int ch = 0; ch < 8; ch++) {
    WAITV(0);
    BAR();
    if (ch < 7) {
#pragma unroll
      for (int i = 0; i < 4; i++) glds16(src + (ch + 1) * 32768 + i * 1024, dst + ((ch + 1) & 1) * 32768 + i * 1024);
    }
    const int bo = (ch & 1) * 32768;
    if (ch == 0) {
      f32x16 s = mfma32(*(const bf16x8*)(l16), q0, z);
      s = mfma32x8(*(const u32x2*)(l8), q1, s);
      float t = max16(s);
      t = fmaxf(t, xhalf(t));
      mm = ceilf(t) + ABIAS;
      if (hf) q1 = ext_of(mm);
    } else {
      int dl = __builtin_amdgcn_frexp_expf(o[12]) + (int)ABIAS;
      if (__any(dl > 16)) {
        dl = dl > 0 ? dl : 0;
#pragma unroll
        for (int j = 0; j < 13; j++) o[j] = __builtin_amdgcn_ldexpf(o[j], -dl);
        mm += (float)dl;
        if (hf) q1 = ext_of(mm);
      }
    }
    f32x16 s = mfma32(*(const bf16x8*)(l16 + bo), q0, z);
    s = mfma32x8(*(const u32x2*)(l8 + bo), q1, s);
#pragma unroll
    for (int t = 0; t < 8; t++) {
      f32x16 sn;
      if (t < 7) {
        sn = mfma32(*(const bf16x8*)(l16 + bo + (t + 1) * 4096), q0, z);
        sn = mfma32x8(*(const u32x2*)(l8 + bo + (t + 1) * 4096), q1, sn);
      }
      pv_step(s, o, *(const bf16x8*)(l16 + bo + t * 4096 + 2048), *(const bf16x8*)(l16 + bo + t * 4096 + 3072));
      if (t < 7) s = sn;
    }
  }
  float l = o[12], chk = 0.f;
#pragma unroll
  for (int j = 0; j < 12; j++) chk = fmaf(o[j], 0.f, chk);
  const bool ok = (l > 7.5e-37f) && (l < 1.0e30f) && (chk == 0.f);
  if (!__all(ok)) {
    q1 = hf ? u32x2{0u, 0u} : q1r;
#pragma unroll
    for (int j = 0; j < 16; j++) o[j] = 0.f;
    float rm = -__builtin_inff();
#pragma unroll 1
    for (int t = 0; t < 64; t++) {
      const char* g = kvg + t * 4096;
      f32x16 s = mfma32(*(const bf16x8*)(g + lane * 16), q0, z);
      s = mfma32x8(*(const u32x2*)(g + 1024 + lane * 8), q1, s);
      float tm = max16(s);
      tm = fmaxf(tm, xhalf(tm));
      float mn = fmaxf(rm, tm);
      float al = __builtin_amdgcn_exp2f(rm - mn);
      rm = mn;
#pragma unroll
      for (int j = 0; j < 16; j++) { o[j] *= al; s[j] -= mn; }
      pv_step(s, o, *(const bf16x8*)(g + 2048 + lane * 16), *(const bf16x8*)(g + 3072 + lane * 16));
    }
    l = o[12];
  }
  const float il = 1.0f / l;
  char* act = ws + ACT_OFF + (size_t)mt * NKS3 * 1024;
  *(u32x4*)(act + head * 1024 + lane * 16) = pack8(o, 0, il);
  if (hf == 0) *(u32x2*)(act + (32 + (head >> 2)) * 1024 + (m + 32 * (head & 1)) * 16 + ((head >> 1) & 1) * 8) = pack4(o, 8, il);
}

DEVI void ffn_block(int b, const u16* __restrict__ wg, const u16* __restrict__ wu, char* __restrict__ ws, char* sm) {
  const int tid = threadIdx.x, lane = tid & 63, w = __builtin_amdgcn_readfirstlane(tid >> 6);
  const int t = (b & 7) * 27 + (b >> 3);
  const int hi = t >= 108, tt = t - hi * 108, cg = tt >> 2, rb = hi * 4 + (tt & 3);
  const int wm = w & 3, wn = w >> 2, hf = lane >> 5;
  const char* asrc = ws + XN_OFF + (size_t)(rb * 8 + w) * 40 * 1024 + lane * 16;
  const int wr = 4 * w + (lane >> 4), wch = (lane & 15) ^ (((wr & 3) << 2) | ((wr >> 2) & 3));
  const u16* wsrc = (wch < 8 ? wg : wu) + (size_t)wr * FF + cg * 64 + (wch & 7) * 8;
  auto issue = [&](int st, int buf) {
    char* d = sm + buf * 24576;
    glds16(asrc + st * 2048, d + w * 2048);
    glds16(asrc + st * 2048 + 1024, d + w * 2048 + 1024);
    glds16(wsrc + (size_t)st * 32 * FF, d + 16384 + w * 1024);
  };
  issue(0, 0); issue(1, 1);
  const int q = (lane & 15) >> 2, p = lane & 3, blk = (lane >> 4) & 1;
  int wo[2][2];
#pragma unroll
  for (int gu = 0; gu < 2; gu++)
#pragma unroll
    for (int tq = 0; tq < 2; tq++) {
      int r = 8 * hf + 4 * tq + q, c0 = gu * 8 + 4 * wn + 2 * blk + (p >> 1);
      wo[gu][tq] = 16384 + 256 * r + 16 * (c0 ^ (((r & 3) << 2) | ((r >> 2) & 3))) + 8 * (p & 1);
    }
  const int ao = wm * 4096 + lane * 16;
  f32x16 ag[2], au[2];
#pragma unroll
  for (int j = 0; j < 16; j++) { ag[0][j] = 0.f; ag[1][j] = 0.f; au[0][j] = 0.f; au[1][j] = 0.f; }
  int buf = 0;
#pragma unroll 1
  for (int st = 0; st < 20; st++) {
    if (st < 19) WAITV(3); else WAITV(0);
    BAR();
    if (st < 18) issue(st + 2, buf == 0 ? 2 : buf - 1);
    const char* d = sm + buf * 24576;
#pragma unroll
    for (int ks = 0; ks < 2; ks++) {
      bf16x8 b0 = *(const bf16x8*)(d + ao + ks * 1024), b1 = *(const bf16x8*)(d + ao + 2048 + ks * 1024);
      bf16x8 fg = cat(trrd(d + wo[0][0] + ks * 4096), trrd(d + wo[0][1] + ks * 4096));
      bf16x8 fu = cat(trrd(d + wo[1][0] + ks * 4096), trrd(d + wo[1][1] + ks * 4096));
      ag[0] = mfma32(fg, b0, ag[0]); ag[1] = mfma32(fg, b1, ag[1]);
      au[0] = mfma32(fu, b0, au[0]); au[1] = mfma32(fu, b1, au[1]);
    }
    buf = buf == 2 ? 0 : buf + 1;
  }
#pragma unroll
  for (int i = 0; i < 2; i++) {
#pragma unroll
    for (int j = 0; j < 16; j++) {
      float g = ag[i][j];
      ag[i][j] = g * au[i][j] * __builtin_amdgcn_rcpf(1.0f + __builtin_amdgcn_exp2f(g * -1.4426950408889634f));
    }
    char* hp = ws + ACT_OFF + ((size_t)(rb * 8 + wm * 2 + i) * NKS3 + 40 + cg * 4 + wn * 2) * 1024 + lane * 16;
    *(u32x4*)(hp) = pack8(ag[i], 0, 1.0f);
    *(u32x4*)(hp + 1024) = pack8(ag[i], 8, 1.0f);
  }
}

constexpr int K2_LDS = 73728;
__global__ __launch_bounds__(512, 4) void k2_attn_ffn(const u16* __restrict__ wg, const u16* __restrict__ wu, char* __restrict__ ws, int mode) {
  extern __shared__ __attribute__((aligned(16))) char sm[];
  const int b = blockIdx.x + mode;
  if (b < 256) attn_block(b, ws, sm);
  else ffn_block(b - 256, wg, wu, ws, sm);
}

constexpr int K3_LDS = 147456;
__global__ __launch_bounds__(512, 2) void k3_out(const u16* __restrict__ x, const u16* __restrict__ wo, const u16* __restrict__ wd,
                                                 const char* __restrict__ ws, u16* __restrict__ out) {
  extern __shared__ __attribute__((aligned(16))) char sm[];
  const int tid = threadIdx.x, lane = tid & 63, w = __builtin_amdgcn_readfirstlane(tid >> 6);
  const int b = blockIdx.x, xc = b & 7, ii = b >> 3, rb = (xc >> 1) * 8 + (ii & 7), cn = (xc & 1) * 4 + (ii >> 3);
  const int s0 = w * 9 + (w < 2 ? w : 2), ns = w < 2 ? 10 : 9;
  const char* asrc = ws + ACT_OFF + (size_t)(rb * 2) * NKS3 * 1024 + lane * 16;
  char* base = sm + w * 18432;
  int wr[5], wc[5];
#pragma unroll
  for (int i = 0; i < 5; i++) { int id = i * 64 + lane; wr[i] = id / 10; wc[i] = cn * 80 + (id - wr[i] * 10) * 8; }
  auto issue = [&](int st, int buf) {
    char* d = base + buf * 9216;
    glds16(asrc + st * 2048, d); glds16(asrc + st * 2048 + 1024, d + 1024);
    glds16(asrc + NKS3 * 1024 + st * 2048, d + 2048); glds16(asrc + NKS3 * 1024 + st * 2048 + 1024, d + 3072);
    if (st >= 20) {
#pragma unroll
      for (int i = 0; i < 5; i++) glds16(wd + (size_t)((st - 20) * 32 + wr[i]) * DM + wc[i], d + 4096 + i * 1024);
    } else {
#pragma unroll
      for (int i = 0; i < 5; i++) {
        int r = st < 16 ? 40 * st + 20 * (wr[i] >> 4) + (wr[i] & 15) : 160 * (st - 16) + 20 * (wr[i] >> 2) + 16 + (wr[i] & 3);
        glds16(wo + (size_t)r * DM + wc[i], d + 4096 + i * 1024);
      }
    }
  };
  issue(s0, 0);
  const int g = lane >> 4, q = (lane & 15) >> 2, p = lane & 3;
  const int ao = (g >> 1) * 1024 + ((lane & 15) + 32 * (g & 1)) * 16;
  const int wof = 4096 + (16 * (g >> 1) + 4 * (g & 1) + q) * 160 + 8 * p;
  f32x4 acc[5][4];
#pragma unroll
  for (int i = 0; i < 5; i++)
#pragma unroll
    for (int j = 0; j < 4; j++) acc[i][j] = f32x4{0.f, 0.f, 0.f, 0.f};
#pragma unroll 1
  for (int it = 0; it < ns; it++) {
    if (it + 1 < ns) { issue(s0 + it + 1, (it + 1) & 1); WAITV(9); } else WAITV(0);
    const char* d = base + (it & 1) * 9216;
    bf16x8 bf[4];
#pragma unroll
    for (int j = 0; j < 4; j++) bf[j] = *(const bf16x8*)(d + ao + (j >> 1) * 2048 + (j & 1) * 256);
#pragma unroll
    for (int i = 0; i < 5; i++) {
      bf16x8 af = cat(trrd(d + wof + i * 32), trrd(d + wof + i * 32 + 1280));
#pragma unroll
      for (int j = 0; j < 4; j++) acc[i][j] = mfma16(af, bf[j], acc[i][j]);
    }
    asm volatile("s_waitcnt lgkmcnt(0)" ::: "memory");
  }
  BAR();
  char* red = sm + (w & 3) * 20480 + lane * 16;
  if (w >= 4) {
#pragma unroll
    for (int i = 0; i < 20; i++) *(f32x4*)(red + i * 1024) = acc[i >> 2][i & 3];
  }
  BAR();
  if (w < 4) {
#pragma unroll
    for (int i = 0; i < 20; i++) *(f32x4*)(red + i * 1024) = acc[i >> 2][i & 3] + *(const f32x4*)(red + i * 1024);
  }
  BAR();
  for (int id = tid; id < 1280; id += 512) {
    const char* r = sm + id * 16;
    f32x4 v = *(const f32x4*)(r) + *(const f32x4*)(r + 20480) + *(const f32x4*)(r + 40960) + *(const f32x4*)(r + 61440);
    int i = id >> 6, l = id & 63;
    size_t o = (size_t)(rb * 64 + (i & 3) * 16 + (l & 15)) * DM + cn * 80 + (i >> 2) * 16 + (l >> 4) * 4;
    u32x2 xv = *(const u32x2*)(x + o);
    *(u32x2*)(out + o) = u32x2{pk2(v[0] + bflo(xv[0]), v[1] + bfhi(xv[0])), pk2(v[2] + bflo(xv[1]), v[3] + bfhi(xv[1]))};
  }
}

extern "C" void kernel_launch(void* const* d_in, const int* in_sizes, int n_in, void* d_out, int out_size, void* d_ws, size_t ws_size,
                              hipStream_t stream) {
  const u16* x = (const u16*)d_in[0];
  const u16* wq = (const u16*)d_in[1];
  const u16* wk = (const u16*)d_in[2];
  const u16* wv = (const u16*)d_in[3];
  const u16* wo = (const u16*)d_in[4];
  const u16* wg = (const u16*)d_in[6];
  const u16* wu = (const u16*)d_in[7];
  const u16* wd = (const u16*)d_in[8];
  char* ws = (char*)d_ws;
  (void)hipFuncSetAttribute((const void*)k1_ln_qkv, hipFuncAttributeMaxDynamicSharedMemorySize, K1_LDS);
  (void)hipFuncSetAttribute((const void*)k2_attn_ffn, hipFuncAttributeMaxDynamicSharedMemorySize, K2_LDS);
  (void)hipFuncSetAttribute((const void*)k3_out, hipFuncAttributeMaxDynamicSharedMemorySize, K3_LDS);
  k1_ln_qkv<<<256, 256, K1_LDS, stream>>>(x, wq, wk, wv, ws);
  k2_attn_ffn<<<256 + 216, 512, K2_LDS, stream>>>(wg, wu, ws, 0);
  k3_out<<<256, 512, K3_LDS, stream>>>(x, wo, wd, ws, (u16*)d_out);
}
```

```cpp
#include <hip/hip_runtime.h>
#include <hip/hip_bf16.h>

typedef unsigned short u16;
typedef __attribute__((ext_vector_type(8))) __bf16 bf16x8;
typedef __attribute__((ext_vector_type(2))) __bf16 bf16x2;
typedef __attribute__((ext_vector_type(4))) short s16x4;
typedef __attribute__((ext_vector_type(8))) short s16x8;
typedef __attribute__((ext_vector_type(16))) float f32x16;
typedef __attribute__((ext_vector_type(4))) float f32x4;
typedef __attribute__((ext_vector_type(2))) float f32x2;
typedef __attribute__((ext_vector_type(4))) unsigned u32x4;
typedef __attribute__((ext_vector_type(2))) unsigned u32x2;
typedef __attribute__((address_space(3))) s16x4* lds_s16x4_p;
typedef __attribute__((address_space(3))) void* lds_void_p;

constexpr int DM = 640, FF = 1728;
constexpr int NKS3 = 148;
constexpr size_t XN_OFF  = 0;
constexpr size_t QF0_OFF = XN_OFF + 64 * 40 * 1024;
constexpr size_t WQP_OFF = QF0_OFF;
constexpr size_t QF1_OFF = QF0_OFF + 64 * 32 * 1024;
constexpr size_t KV_OFF  = QF1_OFF + 64 * 32 * 256;
constexpr size_t ACT_OFF = KV_OFF + 64 * 4096;
constexpr size_t WPK_OFF = ACT_OFF + 64 * NKS3 * 1024;

#define DEVI __device__ __forceinline__
#define STAMP(k, i) do {} while (0)
DEVI unsigned pk2(float a, float b) { f32x2 v = {a, b}; return __builtin_bit_cast(unsigned, __builtin_convertvector(v, bf16x2)); }
DEVI float bflo(unsigned u) { return __builtin_bit_cast(float, u << 16); }
DEVI float bfhi(unsigned u) { return __builtin_bit_cast(float, u & 0xffff0000u); }
DEVI void gldsv(const void* g, unsigned lds) { asm volatile("s_mov_b32 m0, %1\n\ts_nop 0\n\tglobal_load_lds_dwordx4 %0, off" ::"v"(g), "s"(lds) : "memory"); }
#define GLDSS2(voff, sbase, lds) asm volatile("s_mov_b32 m0, %2\n\ts_nop 0\n\tglobal_load_lds_dwordx4 %0, %1\n\tglobal_load_lds_dwordx4 %0, %1 offset:1024" ::"v"(voff), "s"(sbase), "s"(lds) : "memory")
#define GLDSS1(voff, sbase, lds) asm volatile("s_mov_b32 m0, %2\n\ts_nop 0\n\tglobal_load_lds_dwordx4 %0, %1" ::"v"(voff), "s"(sbase), "s"(lds) : "memory")
DEVI unsigned ldsoff(const char* p) { return (unsigned)(size_t)(lds_void_p)p; }
DEVI void st16(void* p, u32x4 v) { asm volatile("global_store_dwordx4 %0, %1, off sc0 sc1\n\ts_nop 1" ::"v"(p), "v"(v) : "memory"); }
DEVI void st8(void* p, u32x2 v) { *(u32x2*)p = v; }
#define SB() __builtin_amdgcn_sched_barrier(0)
DEVI s16x4 trrd(const char* l) { return __builtin_amdgcn_ds_read_tr16_b64_v4i16((lds_s16x4_p)l); }
DEVI bf16x8 cat(s16x4 a, s16x4 b) { return __builtin_bit_cast(bf16x8, __builtin_shufflevector(a, b, 0, 1, 2, 3, 4, 5, 6, 7)); }
DEVI f32x16 mfma32(bf16x8 a, bf16x8 b, f32x16 c) { return __builtin_amdgcn_mfma_f32_32x32x16_bf16(a, b, c, 0, 0, 0); }
DEVI f32x4 mfma16(bf16x8 a, bf16x8 b, f32x4 c) { return __builtin_amdgcn_mfma_f32_16x16x32_bf16(a, b, c, 0, 0, 0); }
DEVI f32x16 mfma32x8(u32x2 a, u32x2 b, f32x16 c) { return __builtin_amdgcn_mfma_f32_32x32x8bf16_1k(__builtin_bit_cast(s16x4, a), __builtin_bit_cast(s16x4, b), c, 0, 0, 0); }
DEVI float xhalf(float v) { return __shfl_xor(v, 32); }
#define WAITV(n) asm volatile("s_waitcnt vmcnt(" #n ")" ::: "memory")
#define BAR() do { asm volatile("s_waitcnt lgkmcnt(0)" ::: "memory"); __builtin_amdgcn_s_barrier(); asm volatile("" ::: "memory"); } while (0)
DEVI u32x4 pack8(const f32x16& a, int o, float s) {
  u32x4 r; r[0] = pk2(a[o] * s, a[o + 1] * s); r[1] = pk2(a[o + 2] * s, a[o + 3] * s); r[2] = pk2(a[o + 4] * s, a[o + 5] * s); r[3] = pk2(a[o + 6] * s, a[o + 7] * s); return r;
}
DEVI u32x2 pack4(const f32x16& a, int o, float s) { u32x2 r; r[0] = pk2(a[o] * s, a[o + 1] * s); r[1] = pk2(a[o + 2] * s, a[o + 3] * s); return r; }

constexpr int K1_STAT = 65536, K1_LDS = 67584;
__global__ __launch_bounds__(512, 2) void k1_ln_kv(const u16* __restrict__ x, const u16* __restrict__ wq, const u16* __restrict__ wk,
                                                   const u16* __restrict__ wv, char* __restrict__ ws) {
  extern __shared__ __attribute__((aligned(16))) char sm[];
  const int tid = threadIdx.x, lane = tid & 63, w = __builtin_amdgcn_readfirstlane(tid >> 6);
  const int b = blockIdx.x;
  STAMP(0, 0);
  if (b >= 64) {
    const int pb = b - 64;
#pragma unroll
    for (int i = 0; i < 2; i++) {
      const int col = tid + 512 * i;
      if (col < 640) {
        const u16* src = wq + (size_t)pb * 8 * DM + col;
        u32x4 v;
#pragma unroll
        for (int j = 0; j < 4; j++) v[j] = (unsigned)src[2 * j * DM] | ((unsigned)src[(2 * j + 1) * DM] << 16);
        const int hd = col / 20, d = col - hd * 20;
        const int hl = hd & 7, tile = d < 16 ? hl >> 1 : 4, r = d < 16 ? (hl & 1) * 16 + d : hl * 4 + d - 16;
        *(u32x4*)(ws + WQP_OFF + (size_t)(((hd >> 3) * 5 + tile) * 40 + (pb >> 1)) * 1024 + (r + (pb & 1) * 32) * 16) = v;
      }
    }
    return;
  }
  const int m = lane & 31, hf = lane >> 5, mt = b;
  u32x4 xf[5], wl[2][4];
  {
    const u16* xp = x + (size_t)(mt * 32 + m) * DM + w * 80 + hf * 8;
#pragma unroll
    for (int s = 0; s < 5; s++) xf[s] = *(const u32x4*)(xp + 16 * s);
#pragma unroll
    for (int i = 0; i < 4; i++) {
      const int id = i < 3 ? i * 64 + lane : 192 + (lane & 7);
      wl[0][i] = *(const u32x4*)(wk + w * 1600 + id * 8);
      wl[1][i] = *(const u32x4*)(wv + w * 1600 + id * 8);
    }
  }
  STAMP(0, 1);
  float s1 = 0.f, s2 = 0.f;
  const bf16x2 one2 = {(__bf16)1.0f, (__bf16)1.0f};
#pragma unroll
  for (int s = 0; s < 5; s++)
#pragma unroll
    for (int j = 0; j < 4; j++) {
      const unsigned u0 = xf[s][j];
      const bf16x2 v0 = __builtin_bit_cast(bf16x2, u0);
      s1 = __builtin_amdgcn_fdot2_f32_bf16(v0, one2, s1, false);
      s2 = __builtin_amdgcn_fdot2_f32_bf16(v0, v0, s2, false);
    }
  s1 += xhalf(s1); s2 += xhalf(s2);
  if (hf == 0) *(f32x2*)(sm + K1_STAT + (m * 8 + w) * 8) = f32x2{s1, s2};
  char* wl0 = sm + w * 8192;
#pragma unroll
  for (int i = 0; i < 4; i++) {
    const int id = i < 3 ? i * 64 + lane : 192 + (lane & 7);
    *(u32x4*)(wl0 + id * 16) = wl[0][i];
    *(u32x4*)(wl0 + 4096 + id * 16) = wl[1][i];
  }
  STAMP(0, 2);
  BAR();
  {
    const f32x4* sp = (const f32x4*)(sm + K1_STAT + m * 64);
    const f32x4 o = (sp[0] + sp[1]) + (sp[2] + sp[3]);
    s1 = o[0] + o[2]; s2 = o[1] + o[3];
  }
  const char* a0 = wl0 + (8 * hf + ((lane & 15) >> 2)) * 40 + ((lane >> 4) & 1) * 32 + 8 * (lane & 3);
  bf16x8 wf[5][2];
#pragma unroll
  for (int s = 0; s < 5; s++) {
    wf[s][0] = cat(trrd(a0 + s * 640), trrd(a0 + s * 640 + 160));
    wf[s][1] = cat(trrd(a0 + 4096 + s * 640), trrd(a0 + 4096 + s * 640 + 160));
  }
  const float mean = s1 * (1.0f / DM);
  const float var = fmaxf(s2 * (1.0f / DM) - mean * mean, 0.f);
  const float rstd = 1.0f / sqrtf(var + 1e-5f), nb = -mean * rstd;
  f32x16 ak, av;
#pragma unroll
  for (int j = 0; j < 16; j++) { ak[j] = 0.f; av[j] = 0.f; }
  char* xn = ws + XN_OFF + (size_t)(mt * 40 + w * 5) * 1024 + lane * 16;
#pragma unroll
  for (int s = 0; s < 5; s++) {
#pragma unroll
    for (int j = 0; j < 4; j++) xf[s][j] = pk2(fmaf(bflo(xf[s][j]), rstd, nb), fmaf(bfhi(xf[s][j]), rstd, nb));
    st16(xn + s * 1024, xf[s]);
    const bf16x8 bv = __builtin_bit_cast(bf16x8, xf[s]);
    ak = mfma32(wf[s][0], bv, ak);
    av = mfma32(bv, wf[s][1], av);
  }
  STAMP(0, 3);
#pragma unroll
  for (int g = 0; g < 4; g++) {
    *(f32x4*)(wl0 + g * 1024 + lane * 16) = f32x4{ak[4 * g], ak[4 * g + 1], ak[4 * g + 2], ak[4 * g + 3]};
    *(f32x4*)(wl0 + 4096 + g * 1024 + lane * 16) = f32x4{av[4 * g], av[4 * g + 1], av[4 * g + 2], av[4 * g + 3]};
  }
  BAR();
  f32x4 r = {0.f, 0.f, 0.f, 0.f};
#pragma unroll
  for (int o = 0; o < 8; o++) r += *(const f32x4*)(sm + o * 8192 + w * 1024 + lane * 16);
  u32x2 pr = {pk2(r[0], r[1]), pk2(r[2], r[3])};
  char* kv = ws + KV_OFF + (size_t)mt * 4096;
  if (w < 2) st8(kv + lane * 16 + w * 8, pr);
  else if (w == 2) st8(kv + 1024 + lane * 8, hf ? u32x2{0x3f803f80u, 0x00003f80u} : pr);
  else if (w >= 4) {
    const unsigned c = (m == 24 || m == 28) ? 0x3f803f80u : 0u;
    st8(kv + 2048 + ((w >> 1) & 1) * 1024 + lane * 16 + (w & 1) * 8, m < 20 ? pr : u32x2{c, c});
  }
  STAMP(0, 4);
}

constexpr float ABIAS = 64.f;
DEVI u32x2 ext_of(float m) {
  float n = -m;
  unsigned h = __builtin_bit_cast(unsigned, n) & 0xffff0000u; float r = n - __builtin_bit_cast(float, h);
  unsigned md = __builtin_bit_cast(unsigned, r) & 0xffff0000u; float r2 = r - __builtin_bit_cast(float, md);
  return u32x2{(h >> 16) | md, __builtin_bit_cast(unsigned, r2) >> 16};
}
DEVI float max16(const f32x16& s) {
  float a = fmaxf(fmaxf(s[0], s[1]), s[2]);
#pragma unroll
  for (int j = 3; j < 15; j += 2) a = fmaxf(fmaxf(a, s[j]), s[j + 1]);
  return fmaxf(a, s[15]);
}
DEVI void pv_step(f32x16& s, f32x16& o, bf16x8 v0, bf16x8 v1) {
#pragma unroll
  for (int j = 0; j < 16; j++) s[j] = __builtin_amdgcn_exp2f(s[j]);
  o = mfma32(v0, __builtin_bit_cast(bf16x8, pack8(s, 0, 1.0f)), o);
  o = mfma32(v1, __builtin_bit_cast(bf16x8, pack8(s, 8, 1.0f)), o);
}
#define PIN(x) asm volatile("" : "+v"(x))
#define EXPG(j, da, db) do { unsigned pa, pb; float n0, n1, n2, n3; \
    asm volatile("v_exp_f32 %2, %6\n\tv_exp_f32 %3, %7\n\tv_cvt_pk_bf16_f32 %0, %10, %11\n\tv_cvt_pk_bf16_f32 %1, %12, %13\n\tv_exp_f32 %4, %8\n\tv_exp_f32 %5, %9" \
                 : "=&v"(pa), "=&v"(pb), "=&v"(n0), "=&v"(n1), "=&v"(n2), "=&v"(n3) \
                 : "v"(s[j]), "v"(s[j + 1]), "v"(s[j + 2]), "v"(s[j + 3]), "v"(e0), "v"(e1), "v"(e2), "v"(e3)); \
    da = pa; db = pb; e0 = n0; e1 = n1; e2 = n2; e3 = n3; SB(); } while (0)
DEVI void attn_block(int b, char* __restrict__ ws, char* sm) {
  const int tid = threadIdx.x, lane = tid & 63, w = __builtin_amdgcn_readfirstlane(tid >> 6);
  const int ii = b >> 3, mt = (b & 7) * 8 + (ii & 7), head = (ii >> 3) * 8 + w;
  const int m = lane & 31, hf = lane >> 5;
  const char* kvg = ws + KV_OFF;
  const unsigned voff = w * 2048 + lane * 16, ldst = ldsoff(sm) + w * 2048;
  {
    const char* xg = ws + XN_OFF + (size_t)(mt * 40 + w * 5) * 1024;
    const unsigned xo = lane * 16, xl = ldsoff(sm) + 32768 + w * 5120;
    GLDSS2(xo, xg, xl); GLDSS2(xo, xg + 2048, xl + 2048); GLDSS1(xo, xg + 4096, xl + 4096);
  }
  GLDSS2(voff, kvg, ldst);
  GLDSS2(voff, kvg + 16384, ldst + 16384);
  f32x16 z, o;
#pragma unroll
  for (int j = 0; j < 16; j++) { z[j] = 0.f; o[j] = 0.f; }
  const char* l16 = sm + lane * 16;
  const char* l8 = sm + 1024 + lane * 8;
  bf16x8 q0;
  u32x2 q1r;
  {
    const int qt = w >> 1, kh = w & 1;
    const char* wm = ws + WQP_OFF + (size_t)(((ii >> 3) * 5 + qt) * 40 + kh * 20) * 1024 + lane * 16;
    const char* wx = ws + WQP_OFF + (size_t)(((ii >> 3) * 5 + 4) * 40 + kh * 20 + qt * 5) * 1024 + lane * 16;
    const char* xs = l16 + 32768 + kh * 20480;
    auto ko = [&](int s) { return (((s / 5 + qt) & 3) * 5 + s % 5) * 1024; };
    auto fr = [&](int i) { return i < 10 ? ((i & 1) ? wx + (i >> 1) * 1024 : wm + ko(i >> 1)) : wm + ko(i - 5); };
    bf16x8 wq[14], xb[2];
#pragma unroll
    for (int i = 0; i < 14; i++) wq[i] = *(const bf16x8*)fr(i);
    WAITV(18);
    BAR();
    f32x16 qa = z, qx = z;
    xb[0] = *(const bf16x8*)(xs + ko(0));
#pragma unroll
    for (int s = 0; s < 20; s++) {
      if (s < 19) xb[(s + 1) & 1] = *(const bf16x8*)(xs + ko(s + 1));
      SB();
      const int i = s < 5 ? 2 * s : s + 5;
      qa = mfma32(wq[i % 14], xb[s & 1], qa);
      if (s < 5) qx = mfma32(wq[(i + 1) % 14], xb[s & 1], qx);
      SB();
      if (i + 14 < 25) wq[i % 14] = *(const bf16x8*)fr(i + 14);
      if (s < 5) wq[(i + 1) % 14] = *(const bf16x8*)fr(i + 15);
    }
    BAR();
    char* sc = sm + 32768 + lane * 16;
#pragma unroll
    for (int g = 0; g < 2; g++)
      *(f32x4*)(sc + w * 2048 + g * 1024) = kh ? f32x4{qa[4 * g], qa[4 * g + 1], qa[4 * g + 2], qa[4 * g + 3]}
                                               : f32x4{qa[8 + 4 * g], qa[9 + 4 * g], qa[10 + 4 * g], qa[11 + 4 * g]};
#pragma unroll
    for (int g = 0; g < 4; g++) *(f32x4*)(sc + 16384 + w * 4096 + g * 1024) = f32x4{qx[4 * g], qx[4 * g + 1], qx[4 * g + 2], qx[4 * g + 3]};
    BAR();
    const float QS = 0.22360679774997896f * 1.4426950408889634f;
    const f32x4 t0 = *(const f32x4*)(sc + (w ^ 1) * 2048), t1 = *(const f32x4*)(sc + (w ^ 1) * 2048 + 1024);
    f32x4 tx = {0.f, 0.f, 0.f, 0.f};
#pragma unroll
    for (int o = 0; o < 8; o++) tx += *(const f32x4*)(sm + 49152 + o * 4096 + qt * 1024 + (m + 32 * kh) * 16);
#pragma unroll
    for (int j = 0; j < 4; j++) { qx[j] = (kh ? qa[8 + j] : qa[j]) + t0[j]; qx[4 + j] = (kh ? qa[12 + j] : qa[4 + j]) + t1[j]; qx[8 + j] = tx[j]; }
    q0 = __builtin_bit_cast(bf16x8, pack8(qx, 0, QS));
    q1r = pack4(qx, 8, QS);
  }
  u32x2 q1 = hf ? u32x2{0u, 0u} : q1r;
  STAMP(1, 1);
  bf16x8 k0 = *(const bf16x8*)(l16), v0 = *(const bf16x8*)(l16 + 2048), v1 = *(const bf16x8*)(l16 + 3072);
  u32x2 k1 = *(const u32x2*)(l8);
  u32x4 p1 = {0u, 0u, 0u, 0u};
  float e0 = 0.f, e1 = 0.f, e2 = 0.f, e3 = 0.f;
  f32x16 s = mfma32(k0, q0, z);
  s = mfma32x8(k1, q1, s);
  k0 = *(const bf16x8*)(l16 + 4096); k1 = *(const u32x2*)(l8 + 4096);
  float mm = max16(s);
  mm = ceilf(fmaxf(mm, xhalf(mm))) + ABIAS;
  if (hf) q1 = ext_of(mm);
#pragma unroll
  for (int j = 0; j < 16; j++) s[j] -= mm;
#pragma unroll 1
  for (int i = 0; i < 16; i++) {
    if (i == 2) STAMP(1, 2);
    if (i & 1) {
      int dl = __builtin_amdgcn_frexp_expf(o[12]) + (int)ABIAS;
      if (__any(dl > 16)) {
        asm volatile("s_nop 1");
        p1[2] = pk2(e0, e1); p1[3] = pk2(e2, e3);
        o = mfma32(v1, __builtin_bit_cast(bf16x8, p1), o);
        p1 = u32x4{0u, 0u, 0u, 0u}; e0 = e1 = e2 = e3 = 0.f;
        dl = __builtin_amdgcn_frexp_expf(o[12]) + (int)ABIAS;
        dl = dl > 0 ? dl : 0;
        const float fd = (float)dl;
#pragma unroll
        for (int j = 0; j < 13; j++) o[j] = __builtin_amdgcn_ldexpf(o[j], -dl);
#pragma unroll
        for (int j = 0; j < 16; j++) s[j] -= fd;
        mm += fd;
        if (hf) q1 = ext_of(mm);
      }
    }
    const int cur = (i & 3) * 16384, nxt = ((i + 1) & 3) * 16384;
#pragma unroll
    for (int t = 0; t < 4; t++) {
      u32x4 p0;
      f32x16 sn;
      PIN(k0); sn = mfma32(k0, q0, z); PIN(sn); SB();
      if (t == 2) {
        if (i < 14) WAITV(2); else WAITV(0);
        __builtin_amdgcn_s_barrier();
        asm volatile("" ::: "memory");
        if (i == 0) GLDSS2(voff, kvg + 32768, ldst + 32768);
        if (i < 13) GLDSS2(voff, kvg + (i + 3) * 16384, ldst + ((i + 3) & 3) * 16384);
      }
      const int ko = t < 2 ? cur + (t + 2) * 4096 : nxt + (t - 2) * 4096;
      const int vo = t < 3 ? cur + (t + 1) * 4096 : nxt;
      k0 = *(const bf16x8*)(l16 + ko);
      EXPG(0, p1[2], p1[3]);
      PIN(p1); o = mfma32(v1, __builtin_bit_cast(bf16x8, p1), o); PIN(o); SB();
      v1 = *(const bf16x8*)(l16 + cur + t * 4096 + 3072);
      EXPG(4, p0[0], p0[1]);
      PIN(k1); sn = mfma32x8(k1, q1, sn); PIN(sn); SB();
      k1 = *(const u32x2*)(l8 + ko);
      EXPG(8, p0[2], p0[3]);
      PIN(p0); o = mfma32(v0, __builtin_bit_cast(bf16x8, p0), o); PIN(o); SB();
      v0 = *(const bf16x8*)(l16 + vo + 2048);
      EXPG(12, p1[0], p1[1]);
      s = sn;
    }
  }
  asm volatile("s_nop 1");
  p1[2] = pk2(e0, e1); p1[3] = pk2(e2, e3);
  o = mfma32(v1, __builtin_bit_cast(bf16x8, p1), o);
  STAMP(1, 3);
  float l = o[12], chk = 0.f;
#pragma unroll
  for (int j = 0; j < 12; j++) chk = fmaf(o[j], 0.f, chk);
  const bool ok = (l > 7.5e-37f) && (l < 1.0e30f) && (chk == 0.f);
  if (!__all(ok)) {
    q1 = hf ? u32x2{0u, 0u} : q1r;
#pragma unroll
    for (int j = 0; j < 16; j++) o[j] = 0.f;
    float rm = -__builtin_inff();
#pragma unroll 1
    for (int t = 0; t < 64; t++) {
      const char* g = kvg + t * 4096;
      f32x16 s = mfma32(*(const bf16x8*)(g + lane * 16), q0, z);
      s = mfma32x8(*(const u32x2*)(g + 1024 + lane * 8), q1, s);
      float tm = max16(s);
      tm = fmaxf(tm, xhalf(tm));
      float mn = fmaxf(rm, tm);
      float al = __builtin_amdgcn_exp2f(rm - mn);
      rm = mn;
#pragma unroll
      for (int j = 0; j < 16; j++) { o[j] *= al; s[j] -= mn; }
      pv_step(s, o, *(const bf16x8*)(g + 2048 + lane * 16), *(const bf16x8*)(g + 3072 + lane * 16));
    }
    l = o[12];
  }
  const float il = 1.0f / l;
  char* act = ws + ACT_OFF + (size_t)mt * NKS3 * 1024;
  st16(act + head * 1024 + lane * 16, pack8(o, 0, il));
  if (hf == 0) st8(act + (32 + (head >> 2)) * 1024 + (m + 32 * (head & 1)) * 16 + ((head >> 1) & 1) * 8, pack4(o, 8, il));
}

#define FSLP 80
DEVI void ffn_block(int b, const u16* __restrict__ wg, const u16* __restrict__ wu, char* __restrict__ ws, char* sm) {
  const int tid = threadIdx.x, lane = tid & 63, w = __builtin_amdgcn_readfirstlane(tid >> 6);
  const int t = (b & 7) * 27 + ((b - 256) >> 3);
  const int hi = t >= 108, tt = t - hi * 108, cg = tt >> 2, rb = hi * 4 + (tt & 3);
  const int wm = w & 3, wn = w >> 2, hf = lane >> 5;
  const int wr = 4 * w + (lane >> 4), wch = (lane & 15) ^ (((wr & 3) << 2) | ((wr >> 2) & 3));
  const u16* wsrc = (wch < 8 ? wg : wu) + (size_t)wr * FF + cg * 64 + (wch & 7) * 8;
  const unsigned lb = ldsoff(sm);
  const char* abase = ws + XN_OFF + (size_t)(rb * 8 + w) * 40 * 1024;
  const unsigned avo = lane * 16;
  auto issue = [&](int st, int buf) {
    GLDSS2(avo, abase + st * 2048, lb + buf * 24576 + w * 2048);
    gldsv(wsrc + (size_t)st * 32 * FF, lb + buf * 24576 + 16384 + w * 1024);
  };
  __builtin_amdgcn_s_sleep(FSLP);
  issue(0, 0); issue(1, 1); issue(2, 2);
  const int q = (lane & 15) >> 2, p = lane & 3, blk = (lane >> 4) & 1;
  int wo[2][2];
#pragma unroll
  for (int gu = 0; gu < 2; gu++)
#pragma unroll
    for (int tq = 0; tq < 2; tq++) {
      int r = 8 * hf + 4 * tq + q, c0 = gu * 8 + 4 * wn + 2 * blk + (p >> 1);
      wo[gu][tq] = 16384 + 256 * r + 16 * (c0 ^ (((r & 3) << 2) | ((r >> 2) & 3))) + 8 * (p & 1);
    }
  const int ao = wm * 4096 + lane * 16;
  f32x16 ag[2], au[2];
#pragma unroll
  for (int j = 0; j < 16; j++) { ag[0][j] = 0.f; ag[1][j] = 0.f; au[0][j] = 0.f; au[1][j] = 0.f; }
  bf16x8 fb0, fb1, fg, fu;
  auto rd = [&](const char* d, int ks) {
    fb0 = *(const bf16x8*)(d + ao + ks * 1024); fb1 = *(const bf16x8*)(d + ao + 2048 + ks * 1024);
    fg = cat(trrd(d + wo[0][0] + ks * 4096), trrd(d + wo[0][1] + ks * 4096));
    fu = cat(trrd(d + wo[1][0] + ks * 4096), trrd(d + wo[1][1] + ks * 4096));
  };
  WAITV(6);
  BAR();
  STAMP(1, 1);
  rd(sm, 0);
  int buf = 0;
#pragma unroll 1
  for (int st = 0; st < 20; st++) {
    if (st == 10) STAMP(1, 2);
    bf16x8 b0 = fb0, b1 = fb1, g = fg, u = fu;
    rd(sm + buf * 24576, 1);
    SB();
    ag[0] = mfma32(g, b0, ag[0]); ag[1] = mfma32(g, b1, ag[1]);
    au[0] = mfma32(u, b0, au[0]); au[1] = mfma32(u, b1, au[1]);
    SB();
    b0 = fb0; b1 = fb1; g = fg; u = fu;
    if (st < 18) WAITV(3); else WAITV(0);
    BAR();
    if (st < 17) issue(st + 3, buf);
    buf = buf == 2 ? 0 : buf + 1;
    rd(sm + buf * 24576, 0);
    SB();
    ag[0] = mfma32(g, b0, ag[0]); ag[1] = mfma32(g, b1, ag[1]);
    au[0] = mfma32(u, b0, au[0]); au[1] = mfma32(u, b1, au[1]);
    SB();
  }
  STAMP(1, 3);
#pragma unroll
  for (int i = 0; i < 2; i++) {
#pragma unroll
    for (int j = 0; j < 16; j++) {
      float g = ag[i][j];
      ag[i][j] = g * au[i][j] * __builtin_amdgcn_rcpf(1.0f + __builtin_amdgcn_exp2f(g * -1.4426950408889634f));
    }
    char* hp = ws + ACT_OFF + ((size_t)(rb * 8 + wm * 2 + i) * NKS3 + 40 + cg * 4 + wn * 2) * 1024 + lane * 16;
    st16(hp, pack8(ag[i], 0, 1.0f));
    st16(hp + 1024, pack8(ag[i], 8, 1.0f));
  }
}

DEVI void pack_block(int pb, const u16* __restrict__ wo, const u16* __restrict__ wd, char* __restrict__ ws, char* sm) {
  const int tid = threadIdx.x, lane = tid & 63, w = tid >> 6;
  u32x4 v[2][5];
#pragma unroll
  for (int h = 0; h < 2; h++) {
    const int st = 2 * pb + h;
#pragma unroll
    for (int i = 0; i < 5; i++) {
      const int id = tid + 512 * i, r = id / 80, cc = id - r * 80;
      const u16* src = st < 16 ? wo + (size_t)(40 * st + r + 4 * (r >> 4)) * DM
                     : st < 20 ? wo + (size_t)(160 * (st - 16) + 20 * (r >> 2) + 16 + (r & 3)) * DM
                               : wd + (size_t)(32 * (st - 20) + r) * DM;
      v[h][i] = *(const u32x4*)(src + cc * 8);
    }
  }
  const int g = lane >> 4, q = (lane & 15) >> 2, p = lane & 3;
  const char* d = sm + (16 * (g >> 1) + 4 * (g & 1) + q) * 1312 + 8 * p + w * 160;
#pragma unroll
  for (int h = 0; h < 2; h++) {
    if (h) __syncthreads();
#pragma unroll
    for (int i = 0; i < 5; i++) {
      const int id = tid + 512 * i, r = id / 80, cc = id - r * 80;
      *(u32x4*)(sm + r * 1312 + cc * 16) = v[h][i];
    }
    __syncthreads();
    char* dst = ws + WPK_OFF + (size_t)((2 * pb + h) * 40 + w * 5) * 1024 + lane * 16;
#pragma unroll
    for (int i = 0; i < 5; i++) st16(dst + i * 1024, __builtin_bit_cast(u32x4, cat(trrd(d + i * 32), trrd(d + i * 32 + 8 * 1312))));
  }
}

constexpr int K2_LDS = 81920;
__global__ __launch_bounds__(512, 4) void k2_attn_ffn(const u16* __restrict__ wg, const u16* __restrict__ wu, const u16* __restrict__ wo,
                                                      const u16* __restrict__ wd, char* __restrict__ ws) {
  extern __shared__ __attribute__((aligned(16))) char sm[];
  const int b = blockIdx.x;
  STAMP(1, 0);
  if (b < 256) attn_block(b, ws, sm);
  else if (b < 472) ffn_block(b, wg, wu, ws, sm);
  else pack_block(b - 472, wo, wd, ws, sm);
  STAMP(1, 4);
}

constexpr int K3_LDS = 163840;
#define K3D 3
__global__ __launch_bounds__(512, 2) void k3_out(const u16* __restrict__ x, const char* __restrict__ ws, u16* __restrict__ out) {
  extern __shared__ __attribute__((aligned(16))) char sm[];
  const int tid = threadIdx.x, lane = tid & 63, w = __builtin_amdgcn_readfirstlane(tid >> 6);
  const int b = blockIdx.x, xc = b & 7, ii = b >> 3, rb = (xc >> 1) * 8 + (ii & 7), cn = (xc & 1) * 4 + (ii >> 3);
  STAMP(2, 0);
  const int st0 = 9 * w + (w < 2 ? w : 2);
  const int g = lane >> 4;
  const char* ap = ws + ACT_OFF + (size_t)(rb * 2) * NKS3 * 1024 + st0 * 2048 + ((g >> 1) * 1024 + ((lane & 15) + 32 * (g & 1)) * 16);
  const char* wp = ws + WPK_OFF + (size_t)(st0 * 40 + cn * 5) * 1024 + lane * 16;
  bf16x8 af[K3D][4], wf[K3D][5];
  auto issue = [&](int j) {
    wf[j % K3D][0] = *(const bf16x8*)(wp + j * 40960);
#pragma unroll
    for (int m = 0; m < 4; m++) af[j % K3D][m] = *(const bf16x8*)(ap + j * 2048 + (m >> 1) * NKS3 * 1024 + (m & 1) * 256);
#pragma unroll
    for (int i = 1; i < 5; i++) wf[j % K3D][i] = *(const bf16x8*)(wp + j * 40960 + i * 1024);
    SB();
  };
#pragma unroll
  for (int j = 0; j < K3D; j++) issue(j);
  f32x4 acc[5][4];
#pragma unroll
  for (int i = 0; i < 5; i++)
#pragma unroll
    for (int j = 0; j < 4; j++) acc[i][j] = f32x4{0.f, 0.f, 0.f, 0.f};
  u32x2 xv[3];
#pragma unroll
  for (int j = 0; j < 10; j++) {
    if (j == 9 && w >= 2) break;
#pragma unroll
    for (int i = 0; i < 5; i++)
#pragma unroll
      for (int m = 0; m < 4; m++) acc[i][m] = mfma16(wf[j % K3D][i], af[j % K3D][m], acc[i][m]);
    SB();
    if (j == 0) STAMP(2, 1);
    if (j == 5) STAMP(2, 2);
    if (j + K3D < 9 || (j + K3D == 9 && w < 2)) issue(j + K3D);
    if (j + K3D == 9) {
#pragma unroll
      for (int k = 0; k < 3; k++) {
        int id = tid + 512 * k, i = id >> 6, l = id & 63;
        if (id < 1280) xv[k] = *(const u32x2*)(x + (size_t)(rb * 64 + (i & 3) * 16 + (l & 15)) * DM + cn * 80 + (i >> 2) * 16 + (l >> 4) * 4);
      }
      SB();
    }
  }
  STAMP(2, 3);
  char* red = sm + w * 20480 + lane * 16;
#pragma unroll
  for (int i = 0; i < 20; i++) *(f32x4*)(red + i * 1024) = acc[i >> 2][i & 3];
  BAR();
#pragma unroll
  for (int k = 0; k < 3; k++) {
    int id = tid + 512 * k, i = id >> 6, l = id & 63;
    if (id < 1280) {
      const char* r = sm + id * 16;
      f32x4 v = ((*(const f32x4*)(r) + *(const f32x4*)(r + 20480)) + (*(const f32x4*)(r + 40960) + *(const f32x4*)(r + 61440))) +
                ((*(const f32x4*)(r + 81920) + *(const f32x4*)(r + 102400)) + (*(const f32x4*)(r + 122880) + *(const f32x4*)(r + 143360)));
      size_t o = (size_t)(rb * 64 + (i & 3) * 16 + (l & 15)) * DM + cn * 80 + (i >> 2) * 16 + (l >> 4) * 4;
      *(u32x2*)(out + o) = u32x2{pk2(v[0] + bflo(xv[k][0]), v[1] + bfhi(xv[k][0])), pk2(v[2] + bflo(xv[k][1]), v[3] + bfhi(xv[k][1]))};
    }
  }
  STAMP(2, 4);
}

extern "C" void kernel_launch(void* const* d_in, const int* in_sizes, int n_in, void* d_out, int out_size, void* d_ws, size_t ws_size,
                              hipStream_t stream) {
  const u16* x = (const u16*)d_in[0];
  const u16* wq = (const u16*)d_in[1];
  const u16* wk = (const u16*)d_in[2];
  const u16* wv = (const u16*)d_in[3];
  const u16* wo = (const u16*)d_in[4];
  const u16* wg = (const u16*)d_in[6];
  const u16* wu = (const u16*)d_in[7];
  const u16* wd = (const u16*)d_in[8];
  char* ws = (char*)d_ws;
  (void)hipFuncSetAttribute((const void*)k1_ln_kv, hipFuncAttributeMaxDynamicSharedMemorySize, K1_LDS);
  (void)hipFuncSetAttribute((const void*)k2_attn_ffn, hipFuncAttributeMaxDynamicSharedMemorySize, K2_LDS);
  (void)hipFuncSetAttribute((const void*)k3_out, hipFuncAttributeMaxDynamicSharedMemorySize, K3_LDS);
  k1_ln_kv<<<144, 512, K1_LDS, stream>>>(x, wq, wk, wv, ws);
  k2_attn_ffn<<<256 + 216 + 37, 512, K2_LDS, stream>>>(wg, wu, wo, wd, ws);
  k3_out<<<256, 512, K3_LDS, stream>>>(x, ws, (u16*)d_out);
}
```
